```python
import torch

def _softmax(x, axis=-1):
    return torch.softmax(x, dim=axis)
def _gelu(x):
    return torch.nn.functional.gelu(x, approximate="tanh")
def _silu(x):
    return torch.nn.functional.silu(x)
def _sigmoid(x):
    return torch.sigmoid(x)
def _layernorm(x, axis=-1, eps=1e-5):
    m = x.mean(dim=axis, keepdim=True)
    v = x.var(dim=axis, keepdim=True, unbiased=False)
    return (x - m) / torch.sqrt(v + eps)
def _scatter_add(tgt, idx, src, axis=0):
    return tgt.clone().index_add_(axis, idx, src)
def _segment_sum(src, seg, n):
    out = torch.zeros((n,) + src.shape[1:], dtype=src.dtype)
    return out.index_add_(0, seg, src)
def _pad(x, pw):
    flat = tuple(v for pair in reversed(pw) for v in pair)
    return torch.nn.functional.pad(x, flat)

kernel_name = 'standalone__grn__sampling'

def setup_inputs():

    _w = (lambda a: (lambda t: t.to(torch.bfloat16) if t.is_floating_point() else t)(torch.as_tensor(a)))
    B, S, D = 1024, 8, 640
    return {
        "x": _w((lambda *s: torch.randn(*s, dtype=torch.bfloat16) * 0.05)(B, S, D)),
        "g": _w((lambda *s: torch.randn(*s, dtype=torch.bfloat16) * 0.4)(D) * 0.1),
        "b": _w((lambda *s: torch.randn(*s, dtype=torch.bfloat16) * 0.1)(D) * 0.1),
    }

def reference(x, g, b):
    gx = torch.sqrt((x * x).sum(axis=-2, keepdims=True))
    nx = gx / (gx.mean(axis=-1, keepdims=True) + 1e-6)
    return g * (x * nx) + b + x
```

```cpp
#include <hip/hip_runtime.h>
#include <hip/hip_bf16.h>
#include <stdint.h>

typedef uint32_t u32x4 __attribute__((ext_vector_type(4)));
typedef float f32x2 __attribute__((ext_vector_type(2)));

#define WPB 4

__device__ __forceinline__ float lo_f(uint32_t v) { return __uint_as_float(v << 16); }
__device__ __forceinline__ float hi_f(uint32_t v) { return __uint_as_float(v & 0xffff0000u); }
typedef __bf16 bf16x2_t __attribute__((ext_vector_type(2)));
__device__ __forceinline__ uint32_t pack_bf16(float a, float b) {
    f32x2 v = {a, b};
    bf16x2_t r = __builtin_convertvector(v, bf16x2_t);
    return __builtin_bit_cast(uint32_t, r);
}

__device__ __forceinline__ float wave_sum(float v) {
#pragma unroll
    for (int o = 32; o > 0; o >>= 1) v += __shfl_xor(v, o, 64);
    return v;
}

constexpr int S = 8, D = 640;

__global__ __launch_bounds__(64 * WPB) void grn_kernel(const uint16_t* __restrict__ x,
                                                       const uint16_t* __restrict__ g,
                                                       const uint16_t* __restrict__ b,
                                                       uint16_t* __restrict__ out, int B) {
    const int lane = threadIdx.x & 63;
    const int wave = __builtin_amdgcn_readfirstlane(threadIdx.x >> 6);
    const int batch = blockIdx.x * WPB + wave;
    if (batch >= B) return;
    const uint16_t* xb = x + (size_t)batch * (S * D);
    uint16_t* ob = out + (size_t)batch * (S * D);
    u32x4 xa[S];
    uint32_t xt[S];
#pragma unroll
    for (int s = 0; s < S; ++s) {
        xa[s] = *reinterpret_cast<const u32x4*>(xb + s * D + lane * 8);
        xt[s] = *reinterpret_cast<const uint32_t*>(xb + s * D + 512 + lane * 2);
    }
    u32x4 ga = *reinterpret_cast<const u32x4*>(g + lane * 8);
    uint32_t gt = *reinterpret_cast<const uint32_t*>(g + 512 + lane * 2);
    u32x4 ba = *reinterpret_cast<const u32x4*>(b + lane * 8);
    uint32_t bt = *reinterpret_cast<const uint32_t*>(b + 512 + lane * 2);

    float ss[10];
#pragma unroll
    for (int j = 0; j < 10; ++j) ss[j] = 0.f;
#pragma unroll
    for (int s = 0; s < S; ++s) {
#pragma unroll
        for (int j = 0; j < 4; ++j) {
            float a = lo_f(xa[s][j]), c = hi_f(xa[s][j]);
            ss[2 * j] = fmaf(a, a, ss[2 * j]);
            ss[2 * j + 1] = fmaf(c, c, ss[2 * j + 1]);
        }
        float a = lo_f(xt[s]), c = hi_f(xt[s]);
        ss[8] = fmaf(a, a, ss[8]);
        ss[9] = fmaf(c, c, ss[9]);
    }
    float tot = 0.f;
#pragma unroll
    for (int j = 0; j < 10; ++j) {
        ss[j] = __builtin_amdgcn_sqrtf(ss[j]);
        tot += ss[j];
    }
    tot = wave_sum(tot);
    const float inv = __builtin_amdgcn_rcpf(tot * (1.0f / D) + 1e-6f);
    float sc[10], bb[10];
#pragma unroll
    for (int j = 0; j < 4; ++j) {
        sc[2 * j] = fmaf(lo_f(ga[j]) * inv, ss[2 * j], 1.0f);
        sc[2 * j + 1] = fmaf(hi_f(ga[j]) * inv, ss[2 * j + 1], 1.0f);
        bb[2 * j] = lo_f(ba[j]);
        bb[2 * j + 1] = hi_f(ba[j]);
    }
    sc[8] = fmaf(lo_f(gt) * inv, ss[8], 1.0f);
    sc[9] = fmaf(hi_f(gt) * inv, ss[9], 1.0f);
    bb[8] = lo_f(bt);
    bb[9] = hi_f(bt);
#pragma unroll
    for (int s = 0; s < S; ++s) {
        u32x4 o;
#pragma unroll
        for (int j = 0; j < 4; ++j) {
            o[j] = pack_bf16(fmaf(lo_f(xa[s][j]), sc[2 * j], bb[2 * j]),
                             fmaf(hi_f(xa[s][j]), sc[2 * j + 1], bb[2 * j + 1]));
        }
        __builtin_nontemporal_store(o, reinterpret_cast<u32x4*>(ob + s * D + lane * 8));
        *reinterpret_cast<uint32_t*>(ob + s * D + 512 + lane * 2) =
            pack_bf16(fmaf(lo_f(xt[s]), sc[8], bb[8]), fmaf(hi_f(xt[s]), sc[9], bb[9]));
    }
}

extern "C" void kernel_launch(void* const* d_in, const int* in_sizes, int n_in,
                              void* d_out, int out_size, void* d_ws, size_t ws_size,
                              hipStream_t stream) {
    const uint16_t* x = (const uint16_t*)d_in[0];
    const uint16_t* g = (const uint16_t*)d_in[1];
    const uint16_t* b = (const uint16_t*)d_in[2];
    uint16_t* out = (uint16_t*)d_out;
    const int B = in_sizes[0] / (S * D);
    grn_kernel<<<(B + WPB - 1) / WPB, 64 * WPB, 0, stream>>>(x, g, b, out, B);
}
```

```cpp
#include <hip/hip_runtime.h>
#include <hip/hip_bf16.h>
#include <stdint.h>

typedef float f32x2 __attribute__((ext_vector_type(2)));
typedef float f32x4 __attribute__((ext_vector_type(4)));
typedef __bf16 bf16x2_t __attribute__((ext_vector_type(2)));

constexpr int S = 8, D = 640;
constexpr int NTHR = D / 2;
constexpr int ROW_B = D * 2;
constexpr int AUX_NT = 2;

__device__ __forceinline__ f32x2 unpack(uint32_t v) {
    f32x2 r = {__uint_as_float(v << 16), __uint_as_float(v & 0xffff0000u)};
    return r;
}
__device__ __forceinline__ uint32_t pack_bf16(f32x2 v) {
    bf16x2_t r = __builtin_convertvector(v, bf16x2_t);
    return __builtin_bit_cast(uint32_t, r);
}
template <int CTRL, int ROW_MASK>
__device__ __forceinline__ float dpp_add(float v) {
    return v + __builtin_bit_cast(float, __builtin_amdgcn_update_dpp(0, __builtin_bit_cast(int, v), CTRL, ROW_MASK, 0xf, true));
}
__device__ __forceinline__ float wave_sum63(float v) {
    v = dpp_add<0xB1, 0xf>(v);
    v = dpp_add<0x4E, 0xf>(v);
    v = dpp_add<0x141, 0xf>(v);
    v = dpp_add<0x140, 0xf>(v);
    v = dpp_add<0x142, 0xa>(v);
    v = dpp_add<0x143, 0xc>(v);
    return v;
}

__global__ __launch_bounds__(NTHR) void grn_kernel(const uint16_t* __restrict__ x,
                                                   const uint16_t* __restrict__ g,
                                                   const uint16_t* __restrict__ b,
                                                   uint16_t* __restrict__ out) {
    __shared__ __attribute__((aligned(32))) float red[8];
    const int tid = threadIdx.x;
    const size_t base = (size_t)blockIdx.x * (S * D);
    auto rx = __builtin_amdgcn_make_buffer_rsrc((void*)(x + base), 0, S * ROW_B, 0x00020000);
    auto ro = __builtin_amdgcn_make_buffer_rsrc((void*)(out + base), 0, S * ROW_B, 0x00020000);

    const uint32_t gr = *reinterpret_cast<const uint32_t*>(g + tid * 2);
    const uint32_t br = *reinterpret_cast<const uint32_t*>(b + tid * 2);
    uint32_t xr[S];
#pragma unroll
    for (int s = 0; s < S; ++s)
        xr[s] = __builtin_amdgcn_raw_buffer_load_b32(rx, tid * 4 + (s & 3) * ROW_B, (s & 4) * ROW_B, AUX_NT);
    __builtin_amdgcn_sched_barrier(0);

    f32x2 xf[S];
    f32x2 gx;
#pragma unroll
    for (int s = 0; s < S; ++s) {
        xf[s] = unpack(xr[s]);
        gx = s == 0 ? xf[s] * xf[s] : __builtin_elementwise_fma(xf[s], xf[s], gx);
    }
    gx.x = __builtin_amdgcn_sqrtf(gx.x);
    gx.y = __builtin_amdgcn_sqrtf(gx.y);

    const float t = wave_sum63(gx.x + gx.y);
    if ((tid & 63) == 63) red[tid >> 6] = t;
    const f32x2 gs = unpack(gr) * gx;
    __builtin_amdgcn_fence(__ATOMIC_RELEASE, "workgroup", "local");
    __builtin_amdgcn_s_barrier();
    __builtin_amdgcn_fence(__ATOMIC_ACQUIRE, "workgroup", "local");
    const f32x4 r4 = *reinterpret_cast<const f32x4*>(&red[0]);
    const float tot = ((r4.x + r4.y) + (r4.z + r4.w)) + red[4];
    const float inv = __builtin_amdgcn_rcpf(tot * (1.0f / D) + 1e-6f);

    const f32x2 one = {1.0f, 1.0f}, iv = {inv, inv};
    const f32x2 sc = __builtin_elementwise_fma(gs, iv, one);
    const f32x2 bb = unpack(br);
#pragma unroll
    for (int s = 0; s < S; ++s)
        __builtin_amdgcn_raw_buffer_store_b32(pack_bf16(__builtin_elementwise_fma(xf[s], sc, bb)), ro,
                                              tid * 4 + (s & 3) * ROW_B, (s & 4) * ROW_B, AUX_NT);
}

extern "C" void kernel_launch(void* const* d_in, const int* in_sizes, int n_in,
                              void* d_out, int out_size, void* d_ws, size_t ws_size,
                              hipStream_t stream) {
    const int B = in_sizes[0] / (S * D);
    grn_kernel<<<B, NTHR, 0, stream>>>((const uint16_t*)d_in[0], (const uint16_t*)d_in[1], (const uint16_t*)d_in[2],
                                       (uint16_t*)d_out);
}
```

```cpp
#include <hip/hip_runtime.h>
#include <hip/hip_bf16.h>
#include <stdint.h>

typedef float f32x2 __attribute__((ext_vector_type(2)));
typedef float f32x4 __attribute__((ext_vector_type(4)));
typedef __bf16 bf16x2_t __attribute__((ext_vector_type(2)));

constexpr int S = 8, D = 640;
constexpr int NTHR = D / 2;
constexpr int ROW_B = D * 2;
constexpr int AUX_NT = 2;

__device__ __forceinline__ f32x2 unpack(uint32_t v) {
    f32x2 r = {__uint_as_float(v << 16), __uint_as_float(v & 0xffff0000u)};
    return r;
}
__device__ __forceinline__ uint32_t pack_bf16(f32x2 v) {
    bf16x2_t r = __builtin_convertvector(v, bf16x2_t);
    return __builtin_bit_cast(uint32_t, r);
}
template <int CTRL, int ROW_MASK>
__device__ __forceinline__ float dpp_add(float v) {
    return v + __builtin_bit_cast(float, __builtin_amdgcn_update_dpp(0, __builtin_bit_cast(int, v), CTRL, ROW_MASK, 0xf, true));
}
__device__ __forceinline__ float wave_sum63(float v) {
    v = dpp_add<0xB1, 0xf>(v);
    v = dpp_add<0x4E, 0xf>(v);
    v = dpp_add<0x141, 0xf>(v);
    v = dpp_add<0x140, 0xf>(v);
    v = dpp_add<0x142, 0xa>(v);
    v = dpp_add<0x143, 0xc>(v);
    return v;
}

template <int XCD_CHUNK>
__global__ __launch_bounds__(NTHR) void grn_kernel(const uint16_t* __restrict__ x,
                                                   const uint16_t* __restrict__ g,
                                                   const uint16_t* __restrict__ b,
                                                   uint16_t* __restrict__ out) {
    __shared__ __attribute__((aligned(32))) float red[8];
    const int tid = threadIdx.x;
    const uint32_t batch = XCD_CHUNK > 0 ? (blockIdx.x & 7) * XCD_CHUNK + (blockIdx.x >> 3) : blockIdx.x;
    const size_t base = (size_t)batch * (S * D);
    auto rx = __builtin_amdgcn_make_buffer_rsrc((void*)(x + base), 0, S * ROW_B, 0x00020000);
    auto ro = __builtin_amdgcn_make_buffer_rsrc((void*)(out + base), 0, S * ROW_B, 0x00020000);

    const uint32_t gr = *reinterpret_cast<const uint32_t*>(g + tid * 2);
    const uint32_t br = *reinterpret_cast<const uint32_t*>(b + tid * 2);
    uint32_t xr[S];
#pragma unroll
    for (int s = 0; s < S; ++s)
        xr[s] = __builtin_amdgcn_raw_buffer_load_b32(rx, tid * 4 + (s & 3) * ROW_B, (s & 4) * ROW_B, AUX_NT);
    __builtin_amdgcn_sched_barrier(0);

    f32x2 xf[S];
    f32x2 gx;
#pragma unroll
    for (int s = 0; s < S; ++s) {
        xf[s] = unpack(xr[s]);
        gx = s == 0 ? xf[s] * xf[s] : __builtin_elementwise_fma(xf[s], xf[s], gx);
    }
    gx.x = __builtin_amdgcn_sqrtf(gx.x);
    gx.y = __builtin_amdgcn_sqrtf(gx.y);

    const float t = wave_sum63(gx.x + gx.y);
    if ((tid & 63) == 63) red[tid >> 6] = t;
    const f32x2 gs = unpack(gr) * gx;
    __builtin_amdgcn_fence(__ATOMIC_RELEASE, "workgroup", "local");
    __builtin_amdgcn_s_barrier();
    __builtin_amdgcn_fence(__ATOMIC_ACQUIRE, "workgroup", "local");
    const f32x4 r4 = *reinterpret_cast<const f32x4*>(&red[0]);
    const float tot = ((r4.x + r4.y) + (r4.z + r4.w)) + red[4];
    const float inv = __builtin_amdgcn_rcpf(tot * (1.0f / D) + 1e-6f);

    const f32x2 one = {1.0f, 1.0f}, iv = {inv, inv};
    const f32x2 sc = __builtin_elementwise_fma(gs, iv, one);
    const f32x2 bb = unpack(br);
#pragma unroll
    for (int s = 0; s < S; ++s)
        __builtin_amdgcn_raw_buffer_store_b32(pack_bf16(__builtin_elementwise_fma(xf[s], sc, bb)), ro,
                                              tid * 4 + (s & 3) * ROW_B, (s & 4) * ROW_B, AUX_NT);
}

extern "C" void kernel_launch(void* const* d_in, const int* in_sizes, int n_in,
                              void* d_out, int out_size, void* d_ws, size_t ws_size,
                              hipStream_t stream) {
    const int B = in_sizes[0] / (S * D);
    const uint16_t* x = (const uint16_t*)d_in[0];
    const uint16_t* g = (const uint16_t*)d_in[1];
    const uint16_t* b = (const uint16_t*)d_in[2];
    if (B == 8 * 128)
        grn_kernel<128><<<B, NTHR, 0, stream>>>(x, g, b, (uint16_t*)d_out);
    else
        grn_kernel<0><<<B, NTHR, 0, stream>>>(x, g, b, (uint16_t*)d_out);
}
```
